```python
import torch

def _softmax(x, axis=-1):
    return torch.softmax(x, dim=axis)
def _gelu(x):
    return torch.nn.functional.gelu(x, approximate="tanh")
def _silu(x):
    return torch.nn.functional.silu(x)
def _sigmoid(x):
    return torch.sigmoid(x)
def _layernorm(x, axis=-1, eps=1e-5):
    m = x.mean(dim=axis, keepdim=True)
    v = x.var(dim=axis, keepdim=True, unbiased=False)
    return (x - m) / torch.sqrt(v + eps)
def _scatter_add(tgt, idx, src, axis=0):
    return tgt.clone().index_add_(axis, idx, src)
def _segment_sum(src, seg, n):
    out = torch.zeros((n,) + src.shape[1:], dtype=src.dtype)
    return out.index_add_(0, seg, src)
def _pad(x, pw):
    flat = tuple(v for pair in reversed(pw) for v in pair)
    return torch.nn.functional.pad(x, flat)

kernel_name = 'concat_heads__mha_block_cached__compressed_kv_cla__decode'

def setup_inputs():

    _w = (lambda a: (lambda t: t.to(torch.bfloat16) if t.is_floating_point() else t)(torch.as_tensor(a)))
    def _A_setup():
        B, Sq, D = 2, 1, 512
        H, Sk, Dh = 32, 4096, 128
        return {
            "x": _w((lambda *s: torch.randn(*s, dtype=torch.bfloat16) * 1.6)(B, Sq, D)),
            "k_cache": _w((lambda *s: torch.randn(*s, dtype=torch.bfloat16) * 1.6)(B, H, Sk, Dh)),
            "v_cache": _w((lambda *s: torch.randn(*s, dtype=torch.bfloat16) * 0.05)(B, H, Sk, Dh)),
            "wq": _w((lambda *s: torch.randn(*s, dtype=torch.bfloat16) * 1.6)(D, H * Dh) * (D ** -0.5)),
            "wo": _w((lambda *s: torch.randn(*s, dtype=torch.bfloat16) * 0.05)(H * Dh, D) * ((H * Dh) ** -0.5)),
        }
    _A_in = _A_setup()

    def _B_setup():
        B, H, Sq, Sk, D, M = 2, 32, 1, 4096, 128, 512
        Dc = 4 * D
        return {
            "x": _w((lambda *s: torch.randn(*s, dtype=torch.bfloat16) * 3.2)(B, Sq, M)),
            "c_kv": _w((lambda *s: torch.randn(*s, dtype=torch.bfloat16) * 0.1)(B, Sk, Dc)),
            "w_q": _w((lambda *s: torch.randn(*s, dtype=torch.bfloat16) * 6.4)(M, H, D) * (M ** -0.5)),
            "w_uk": _w((lambda *s: torch.randn(*s, dtype=torch.bfloat16) * 6.4)(Dc, H, D) * (Dc ** -0.5)),
            "w_uv": _w((lambda *s: torch.randn(*s, dtype=torch.bfloat16) * 0.1)(Dc, H, D) * (Dc ** -0.5)),
            "w_o": _w((lambda *s: torch.randn(*s, dtype=torch.bfloat16) * 0.1)(H, D, M) * ((H * D) ** -0.5)),
        }
    _B_in = _B_setup()

    return {
        "A_x": _A_in["x"],
        "A_k_cache": _A_in["k_cache"],
        "A_v_cache": _A_in["v_cache"],
        "A_wq": _A_in["wq"],
        "A_wo": _A_in["wo"],
        "B_x": _B_in["x"],
        "B_c_kv": _B_in["c_kv"],
        "B_w_q": _B_in["w_q"],
        "B_w_uk": _B_in["w_uk"],
        "B_w_uv": _B_in["w_uv"],
        "B_w_o": _B_in["w_o"],
    }

def reference(A_x, A_k_cache, A_v_cache, A_wq, A_wo, B_x, B_c_kv, B_w_q, B_w_uk, B_w_uv, B_w_o):
    def _A_ref(x, k_cache, v_cache, wq, wo):
        B, Sq, D = x.shape
        H, Dh = 32, 128
        q = (lambda x, *d: x.permute(*d))((x @ wq).reshape(B, Sq, H, Dh), 0, 2, 1, 3)
        scores = torch.einsum("bhqd,bhkd->bhqk", q, k_cache) * (Dh ** -0.5)
        p = _softmax(scores, axis=-1)
        o = torch.einsum("bhqk,bhkd->bhqd", p, v_cache)
        return (lambda x, *d: x.permute(*d))(o, 0, 2, 1, 3).reshape(B, Sq, H * Dh) @ wo

    def _B_ref(x, c_kv, w_q, w_uk, w_uv, w_o):
        B, Sq, M = x.shape[0], x.shape[1], x.shape[2]
        Sk = c_kv.shape[1]
        H, D = w_q.shape[1], w_q.shape[2]
        q = torch.einsum("bqm,mhd->bhqd", x, w_q)
        k = torch.einsum("bkc,chd->bhkd", c_kv, w_uk)
        v = torch.einsum("bkc,chd->bhkd", c_kv, w_uv)
        scores = torch.einsum("bhqd,bhkd->bhqk", q, k) * (D ** -0.5)
        off = Sk - Sq
        qi = torch.arange(Sq)[:, None]
        kj = torch.arange(Sk)[None, :]
        cmask = kj <= (qi + off)
        scores = torch.where(cmask[None, None], scores, -1e9)
        p = _softmax(scores, axis=-1)
        ctx = torch.einsum("bhqk,bhkd->bhqd", p, v)
        return torch.einsum("bhqd,hdm->bqm", ctx, w_o)

    a = _A_ref(A_x, A_k_cache, A_v_cache, A_wq, A_wo)
    b = _B_ref(B_x, B_c_kv, B_w_q, B_w_uk, B_w_uv, B_w_o)
    return torch.concatenate([a, b], axis=-1)
```

```cpp
#include <hip/hip_runtime.h>
#include <hip/hip_bf16.h>
#include <stdint.h>

typedef unsigned int u32;
typedef unsigned short u16;
typedef __attribute__((ext_vector_type(4))) unsigned u32x4;
typedef __attribute__((ext_vector_type(4))) float f32x4;
typedef __attribute__((ext_vector_type(16))) float f32x16;
typedef __attribute__((ext_vector_type(8))) __bf16 bf16x8;
typedef __attribute__((ext_vector_type(2))) __bf16 bf16x2;
typedef __attribute__((ext_vector_type(4))) short s16x4;

constexpr int NB = 2, NH = 32, SK = 4096, SQ = 1, DH = 128, DM = 512, DC = 512, HD = NH * DH;
constexpr int NBW = 64;
constexpr int SA = 3;
constexpr int NTH = 512, NWV = NTH / 64;
constexpr int SB = 32;
static_assert(NB * SB == NBW && NH * 2 == NBW, "branch-B task maps assume 64 workgroups");
constexpr float SCALE = 0.08838834764831845f;
constexpr float LOG2E = 1.4426950408889634f;

constexpr size_t al256(size_t x) { return (x + 255) & ~(size_t)255; }
constexpr size_t WS_CNT  = 0;
constexpr int CNT_ABS = 0, CNT_BATT = 64, CNT_TICKET = 128, CNT_WORDS = 1024;
constexpr size_t WS_QA   = WS_CNT + (size_t)CNT_WORDS * 4;
constexpr size_t WS_QB   = WS_QA + al256((size_t)NB * HD * 4);
constexpr size_t WS_QABS = WS_QB + al256((size_t)NB * HD * 4);
constexpr size_t QPL = (size_t)NB * NH * DC;
constexpr size_t QABS_BYTES = QPL * 2 * 2;
constexpr size_t WS_AML  = WS_QABS + al256(QABS_BYTES);
constexpr size_t WS_APO  = WS_AML + al256((size_t)NB * NH * SA * 2 * 4);
constexpr size_t WS_BML  = WS_APO + al256((size_t)NB * NH * SA * DH * 4);
constexpr size_t WS_BPO  = WS_BML + al256((size_t)NB * SB * NH * 2 * 4);
constexpr size_t BPO_BYTES = (size_t)NB * SB * NH * DC * 4;
constexpr size_t WS_CTX  = WS_BPO + al256(BPO_BYTES);
constexpr size_t WS_OUTP = WS_CTX + al256((size_t)NB * HD * 4);
constexpr size_t OUTP_BYTES = (size_t)2 * 4 * NH * NB * 128 * 4;
constexpr size_t WS_END  = WS_OUTP + al256(OUTP_BYTES);

__device__ __forceinline__ float bf2f(u16 u) { return __uint_as_float(((u32)u) << 16); }
__device__ __forceinline__ float lo16(u32 w) { return __uint_as_float(w << 16); }
__device__ __forceinline__ float hi16(u32 w) { return __uint_as_float(w & 0xffff0000u); }
__device__ __forceinline__ float rbf(float x) { return __bfloat162float(__float2bfloat16(x)); }
__device__ __forceinline__ u16 f2bf_bits(float x) { __hip_bfloat16 h = __float2bfloat16(x); return *reinterpret_cast<u16*>(&h); }
__device__ __forceinline__ u32 pack_bf16(float lo, float hi) { return (u32)f2bf_bits(lo) | ((u32)f2bf_bits(hi) << 16); }
__device__ __forceinline__ float dot2(u32 k, u32 q, float acc) {
  return __builtin_amdgcn_fdot2_f32_bf16(__builtin_bit_cast(bf16x2, k), __builtin_bit_cast(bf16x2, q), acc, false);
}
template <int CTRL> __device__ __forceinline__ float dppf(float x) {
  return __builtin_bit_cast(float, __builtin_amdgcn_mov_dpp(__builtin_bit_cast(int, x), CTRL, 0xf, 0xf, true));
}
constexpr int D_XOR1 = 0xB1, D_XOR2 = 0x4E, D_XOR7 = 0x141, D_XOR8 = 0x128;
__device__ __forceinline__ float xrow_max(float x) { x = fmaxf(x, __shfl_xor(x, 16)); return fmaxf(x, __shfl_xor(x, 32)); }
__device__ __forceinline__ float xrow_sum(float x) { x = x + __shfl_xor(x, 16); return x + __shfl_xor(x, 32); }
__device__ __forceinline__ u32x4 ld16(const u16* p) { return *reinterpret_cast<const u32x4*>(p); }
__device__ __forceinline__ u32x4 ld16nt(const u16* p) { return __builtin_nontemporal_load(reinterpret_cast<const u32x4*>(p)); }
__device__ __forceinline__ void wave_sync() {
  __builtin_amdgcn_fence(__ATOMIC_RELEASE, "wavefront"); __builtin_amdgcn_wave_barrier(); __builtin_amdgcn_fence(__ATOMIC_ACQUIRE, "wavefront");
}

__device__ __forceinline__ void st16_wt(__amdgpu_buffer_rsrc_t rs, u32 byte_off, u32x4 v) {
  __builtin_amdgcn_raw_buffer_store_b128(v, rs, (int)byte_off, 0, 16);
}
__device__ __forceinline__ void signal_count(u32* cnt) {
  asm volatile("s_waitcnt vmcnt(0)" ::: "memory");
  __syncthreads();
  if (threadIdx.x == 0) __hip_atomic_fetch_add(cnt, 1u, __ATOMIC_RELAXED, __HIP_MEMORY_SCOPE_AGENT);
}
__device__ __forceinline__ void wait_count(u32* cnt, u32 target) {
  if (threadIdx.x == 0) {
    for (u32 spins = 0; __hip_atomic_load(cnt, __ATOMIC_RELAXED, __HIP_MEMORY_SCOPE_AGENT) < target && spins < (1u << 18); ++spins)
      __builtin_amdgcn_s_sleep(4);
    __builtin_amdgcn_fence(__ATOMIC_ACQUIRE, "agent");
    asm volatile("s_waitcnt vmcnt(0)" ::: "memory");
  }
  __syncthreads();
}

#define STAMP(slot) do {} while (0)
#define DBG_PARAM
#define STAMP_ID() do {} while (0)
#define DBG_ARG
__global__ __launch_bounds__(256) void k_zero(u32* __restrict__ cnt) {
  for (int i = threadIdx.x; i < CNT_WORDS; i += 256) cnt[i] = 0u;
}

constexpr int PSTR = 272;
constexpr int LDS_TILES = 0;
constexpr int LDS_P  = 16 * 8192;
constexpr int LDS_MX = LDS_P + 32 * PSTR;
constexpr int LDS_LS = LDS_MX + 512;
constexpr int LDS_B1 = LDS_LS + 512;
constexpr int LDS_MAIN_BYTES = LDS_B1 + 4096 + 8192 + 1024 + 2048;
static_assert(LDS_MAIN_BYTES % 16 == 0 && LDS_MAIN_BYTES <= 160 * 1024, "");

#define NI 4
constexpr int TK = 4 * NI;
__device__ __forceinline__ void role_attnA(int item, const u16* __restrict__ K, const u16* __restrict__ V, const u16* __restrict__ xA,
                                           const u16* __restrict__ wqA, float* __restrict__ pml, float* __restrict__ po, char* smem DBG_PARAM) {
  STAMP(0); STAMP_ID();
  float (*pbuf)[16] = reinterpret_cast<float (*)[16]>(smem);
  float (*mrg)[DH + 4] = reinterpret_cast<float (*)[DH + 4]>(smem + 1024);
  float* xs = reinterpret_cast<float*>(smem + 8192);
  float (*red)[DH] = reinterpret_cast<float (*)[DH]>(smem + 12288);
  float* qs = reinterpret_cast<float*>(smem + 16384);
  const int h = ((item >> 3) / (2 * SA)) * 8 + (item & 7), sub = (item >> 3) % (2 * SA), b = sub / SA, split = sub - b * SA;
  const int bh = b * NH + h;
  const int t0 = (SK / TK) * split / SA, t1 = (SK / TK) * (split + 1) / SA;
  const int tid = threadIdx.x, lane = tid & 63, wave = tid >> 6, r = lane >> 4, c = lane & 15;
  const int nt = (t1 - t0 - wave + NWV - 1) / NWV;
  const size_t base = (size_t)bh * SK * DH + (size_t)r * DH + 8 * c;
  const u16 xw = xA[b * DM + tid];
  u32x4 wq[16];
#pragma unroll
  for (int i = 0; i < 16; ++i) wq[i] = ld16(wqA + (size_t)((tid >> 4) + 32 * i) * HD + h * DH + 8 * c);
  u32x4 Kn[NI], Vn[NI];
#pragma unroll
  for (int i = 0; i < NI; ++i) {
    Kn[i] = ld16nt(K + base + (size_t)((t0 + wave) * TK + 4 * i) * DH);
    Vn[i] = ld16nt(V + base + (size_t)((t0 + wave) * TK + 4 * i) * DH);
  }
  xs[tid] = bf2f(xw);
  __syncthreads();
  {
    float a8[8];
#pragma unroll
    for (int k = 0; k < 8; ++k) a8[k] = 0.f;
#pragma unroll
    for (int i = 0; i < 16; ++i) {
      const float x0 = xs[(tid >> 4) + 32 * i];
#pragma unroll
      for (int k = 0; k < 4; ++k) { a8[2 * k] = fmaf(x0, lo16(wq[i][k]), a8[2 * k]); a8[2 * k + 1] = fmaf(x0, hi16(wq[i][k]), a8[2 * k + 1]); }
    }
#pragma unroll
    for (int k = 0; k < 8; ++k) a8[k] = xrow_sum(a8[k]);
    if (r == 0) {
      *reinterpret_cast<f32x4*>(&red[wave][8 * c]) = f32x4{a8[0], a8[1], a8[2], a8[3]};
      *reinterpret_cast<f32x4*>(&red[wave][8 * c + 4]) = f32x4{a8[4], a8[5], a8[6], a8[7]};
    }
    __syncthreads();
    if (tid < DH) {
      float v = 0.f;
#pragma unroll
      for (int w = 0; w < NWV; ++w) v += red[w][tid];
      qs[tid] = rbf(v);
    }
    __syncthreads();
  }
  u32 qp[4];
#pragma unroll
  for (int k = 0; k < 4; ++k) qp[k] = pack_bf16(qs[8 * c + 2 * k], qs[8 * c + 2 * k + 1]);
  float m = -INFINITY, l = 0.f, acc[8];
#pragma unroll
  for (int k = 0; k < 8; ++k) acc[k] = 0.f;
  const int itok = 2 * ((c >> 3) & 1) + ((c >> 2) & 1);
  for (int it = 0; it < nt; ++it) {
    u32x4 Kc[NI], Vc[NI];
#pragma unroll
    for (int i = 0; i < NI; ++i) { Kc[i] = Kn[i]; Vc[i] = Vn[i]; }
    if (it + 1 < nt) {
      const size_t nb = base + (size_t)(t0 + wave + NWV * (it + 1)) * TK * DH;
#pragma unroll
      for (int i = 0; i < NI; ++i) { Kn[i] = ld16nt(K + nb + (size_t)(4 * i) * DH); Vn[i] = ld16nt(V + nb + (size_t)(4 * i) * DH); }
    }
    float x[NI];
#pragma unroll
    for (int i = 0; i < NI; ++i) {
      float d = 0.f;
#pragma unroll
      for (int k = 0; k < 4; ++k) d = dot2(Kc[i][k], qp[k], d);
      x[i] = d;
    }
#pragma unroll
    for (int i = 0; i < 2; ++i) { const float a = x[i] + dppf<D_XOR8>(x[i]), b = x[i + 2] + dppf<D_XOR8>(x[i + 2]); x[i] = (c & 8) ? b : a; }
    { const float a = x[0] + dppf<D_XOR7>(x[0]), b = x[1] + dppf<D_XOR7>(x[1]); x[0] = (c & 4) ? b : a; }
    x[0] += dppf<D_XOR2>(x[0]);
    const float raw = x[0] + dppf<D_XOR1>(x[0]);
    const float s2 = rbf(rbf(raw) * SCALE) * LOG2E;
    float pm = s2;
    pm = fmaxf(pm, dppf<D_XOR7>(pm));
    pm = fmaxf(pm, dppf<D_XOR8>(pm));
    pm = xrow_max(pm);
    const float mn = fmaxf(m, pm);
    const float alpha = exp2f(m - mn), p = exp2f(s2 - mn);
    l = fmaf(l, alpha, p);
    m = mn;
    pbuf[wave][r * 4 + itok] = p;
    wave_sync();
    const f32x4 p0 = *reinterpret_cast<const f32x4*>(&pbuf[wave][r * 4]);
    wave_sync();
#pragma unroll
    for (int k = 0; k < 8; ++k) acc[k] *= alpha;
#pragma unroll
    for (int i = 0; i < NI; ++i) {
#pragma unroll
      for (int k = 0; k < 4; ++k) {
        acc[2 * k] = fmaf(p0[i], lo16(Vc[i][k]), acc[2 * k]);
        acc[2 * k + 1] = fmaf(p0[i], hi16(Vc[i][k]), acc[2 * k + 1]);
      }
    }
  }
#pragma unroll
  for (int k = 0; k < 8; ++k) acc[k] = xrow_sum(acc[k]);
  l += dppf<D_XOR1>(l); l += dppf<D_XOR2>(l); l += dppf<D_XOR7>(l); l += dppf<D_XOR8>(l);
  l = xrow_sum(l) * (NI / 16.f);
  if (r == 0) {
    *reinterpret_cast<f32x4*>(&mrg[wave][4 + 8 * c]) = f32x4{acc[0], acc[1], acc[2], acc[3]};
    *reinterpret_cast<f32x4*>(&mrg[wave][8 + 8 * c]) = f32x4{acc[4], acc[5], acc[6], acc[7]};
    if (c == 0) { mrg[wave][0] = m; mrg[wave][1] = l; }
  }
  __syncthreads();
  if (tid < DH) {
    float M = mrg[0][0];
#pragma unroll
    for (int w = 1; w < NWV; ++w) M = fmaxf(M, mrg[w][0]);
    float L = 0.f, o = 0.f;
#pragma unroll
    for (int w = 0; w < NWV; ++w) { const float wt = exp2f(mrg[w][0] - M); L = fmaf(wt, mrg[w][1], L); o = fmaf(wt, mrg[w][4 + tid], o); }
    po[((size_t)bh * SA + split) * DH + tid] = o;
    if (tid == 0) { pml[((size_t)bh * SA + split) * 2] = M; pml[((size_t)bh * SA + split) * 2 + 1] = L; }
  }
  STAMP(1);
}

__device__ __forceinline__ u32 img_off(u32 row, u32 ch) { return 2048u * (row >> 3) + 512u * (ch >> 2) + 64u * (row & 7) + 16u * ((ch & 3) ^ ((row >> 2) & 3)); }

__device__ __forceinline__ void role_B(int j, const u16* __restrict__ xB, const u16* __restrict__ wqB, const u16* __restrict__ wuk,
                                       u16* __restrict__ qpl, const u16* __restrict__ C, float* __restrict__ bml, float* __restrict__ bpo,
                                       const u16* __restrict__ wuv, float* __restrict__ ctx, u32* __restrict__ cnt, char* smem DBG_PARAM) {
  const int tid = threadIdx.x, lane = tid & 63, wave = tid >> 6;
  const int h = ((j >> 4) << 3) + (j & 7), dh = (j >> 3) & 1;
  STAMP(0); STAMP_ID();
  {
    constexpr int KPS = SK / SB;
    static_assert(KPS == 128, "single-pass layout assumes 128 keys per split");
    const int b = j >> 5, split = j & 31;
    const int hl = lane >> 5, l31 = lane & 31, l15 = lane & 15, g4 = lane >> 4;
    const int hh = wave & 1, kg = wave >> 1;
    char* tiles = smem + LDS_TILES;
    char* P = smem + LDS_P;
    float (*MX)[32] = reinterpret_cast<float (*)[32]>(smem + LDS_MX);
    float (*LS)[32] = reinterpret_cast<float (*)[32]>(smem + LDS_LS);
    const u16* Cg = C + ((size_t)b * SK + (size_t)split * KPS) * DC;
    u32x4 st[16];
    {
      float (*xs)[512] = reinterpret_cast<float (*)[512]>(smem + LDS_B1);
      float (*red)[2][DH] = reinterpret_cast<float (*)[2][DH]>(smem + LDS_B1 + 4096);
      float (*qs)[DH] = reinterpret_cast<float (*)[DH]>(smem + LDS_B1 + 12288);
      u16* qst = reinterpret_cast<u16*>(smem + LDS_B1 + 13312);
      const u16 xw0 = xB[tid], xw1 = xB[DM + tid];
      u32x4 wq[16], wk[8];
#pragma unroll
      for (int i = 0; i < 16; ++i) wq[i] = ld16(wqB + (size_t)((tid >> 4) + 32 * i) * HD + h * DH + 8 * l15);
#pragma unroll
      for (int i = 0; i < 8; ++i) wk[i] = ld16nt(wuk + ((size_t)(256 * dh + wave * 32 + g4 + 4 * i) * NH + h) * DH + 8 * l15);
      xs[0][tid] = bf2f(xw0); xs[1][tid] = bf2f(xw1);
      __syncthreads();
      STAMP(1);
      if (wave != 0) {
#pragma unroll
        for (int i = 0; i < 16; ++i) { const int n = tid + NTH * i; st[i] = ld16(Cg + (size_t)(n >> 6) * DC + 8 * (n & 63)); }
      }
      float acc[2][8];
#pragma unroll
      for (int k = 0; k < 8; ++k) { acc[0][k] = 0.f; acc[1][k] = 0.f; }
#pragma unroll
      for (int i = 0; i < 16; ++i) {
        const float x0 = xs[0][(tid >> 4) + 32 * i], x1 = xs[1][(tid >> 4) + 32 * i];
#pragma unroll
        for (int k = 0; k < 4; ++k) {
          const float a = lo16(wq[i][k]), bb = hi16(wq[i][k]);
          acc[0][2 * k] = fmaf(x0, a, acc[0][2 * k]); acc[0][2 * k + 1] = fmaf(x0, bb, acc[0][2 * k + 1]);
          acc[1][2 * k] = fmaf(x1, a, acc[1][2 * k]); acc[1][2 * k + 1] = fmaf(x1, bb, acc[1][2 * k + 1]);
        }
      }
#pragma unroll
      for (int bb = 0; bb < 2; ++bb)
#pragma unroll
        for (int k = 0; k < 8; ++k) acc[bb][k] = xrow_sum(acc[bb][k]);
      if (g4 == 0) {
#pragma unroll
        for (int bb = 0; bb < 2; ++bb) {
          *reinterpret_cast<f32x4*>(&red[wave][bb][8 * l15]) = f32x4{acc[bb][0], acc[bb][1], acc[bb][2], acc[bb][3]};
          *reinterpret_cast<f32x4*>(&red[wave][bb][8 * l15 + 4]) = f32x4{acc[bb][4], acc[bb][5], acc[bb][6], acc[bb][7]};
        }
      }
      __syncthreads();
      if (tid < 2 * DH) {
        const int bb = tid >> 7, d = tid & 127;
        float v = 0.f;
#pragma unroll
        for (int w = 0; w < NWV; ++w) v += red[w][bb][d];
        qs[bb][d] = rbf(v);
      }
      __syncthreads();
      STAMP(7);
#pragma unroll
      for (int i = 0; i < 8; ++i)
#pragma unroll
        for (int bb = 0; bb < 2; ++bb) {
          float d = 0.f;
#pragma unroll
          for (int k = 0; k < 4; ++k) { d = fmaf(lo16(wk[i][k]), qs[bb][8 * l15 + 2 * k], d); d = fmaf(hi16(wk[i][k]), qs[bb][8 * l15 + 2 * k + 1], d); }
          d += dppf<D_XOR1>(d); d += dppf<D_XOR2>(d); d += dppf<D_XOR7>(d); d += dppf<D_XOR8>(d);
          if (l15 == 0) {
            const int cl = wave * 32 + g4 + 4 * i;
            const u16 hi = f2bf_bits(d);
            qst[bb * 256 + cl] = hi; qst[512 + bb * 256 + cl] = f2bf_bits(d - bf2f(hi));
          }
        }
      __syncthreads();
      if (wave == 0) {
        const __amdgpu_buffer_rsrc_t rq = __builtin_amdgcn_make_buffer_rsrc(qpl, 0, (int)QABS_BYTES, 0x00020000);
#pragma unroll
        for (int pl = 0; pl < 2; ++pl) {
          const int bb = lane >> 5, c8 = lane & 31, cidx = 256 * dh + 8 * c8;
          const u32 el = (u32)(pl * QPL) + (u32)((((bb * 2 + (h >> 4)) * 16 + (cidx >> 5)) * 64 + (h & 15) + 16 * ((cidx >> 3) & 3)) * 8);
          st16_wt(rq, el * 2, *reinterpret_cast<const u32x4*>(qst + pl * 512 + bb * 256 + 8 * c8));
        }
        asm volatile("s_waitcnt vmcnt(0)" ::: "memory");
        if (lane == 0) __hip_atomic_fetch_add(cnt + CNT_ABS, 1u, __ATOMIC_RELAXED, __HIP_MEMORY_SCOPE_AGENT);
#pragma unroll
        for (int i = 0; i < 16; ++i) { const int n = tid + NTH * i; st[i] = ld16(Cg + (size_t)(n >> 6) * DC + 8 * (n & 63)); }
      } else {
#pragma unroll
        for (int i = 0; i < 16; ++i) {
          const int n = tid + NTH * i, row = n >> 6, chg = n & 63;
          *reinterpret_cast<u32x4*>(tiles + ((row >> 5) * 4 + (chg >> 4)) * 8192 + img_off(row & 31, chg & 15)) = st[i];
        }
      }
      STAMP(2);
      wait_count(cnt + CNT_ABS, NBW);
    }
    bf16x8 qh[16], ql[16];
#pragma unroll
    for (int s = 0; s < 16; ++s) {
      const size_t idx = ((size_t)((b * 2 + hh) * 16 + s) * 64 + lane) * 8;
      qh[s] = __builtin_bit_cast(bf16x8, ld16(qpl + idx)); ql[s] = __builtin_bit_cast(bf16x8, ld16(qpl + QPL + idx));
    }
    if (wave == 0) {
#pragma unroll
      for (int i = 0; i < 16; ++i) {
        const int n = tid + NTH * i, row = n >> 6, chg = n & 63;
        *reinterpret_cast<u32x4*>(tiles + ((row >> 5) * 4 + (chg >> 4)) * 8192 + img_off(row & 31, chg & 15)) = st[i];
      }
    }
    __syncthreads();
    f32x4 sa[2];
#pragma unroll
    for (int ks2 = 0; ks2 < 2; ++ks2) {
      sa[ks2] = f32x4{0.f, 0.f, 0.f, 0.f};
#pragma unroll
      for (int s = 0; s < 16; ++s) {
        const bf16x8 cf = *reinterpret_cast<const bf16x8*>(tiles + (kg * 4 + (s >> 2)) * 8192 + img_off(16 * ks2 + l15, 4 * (s & 3) + g4));
        sa[ks2] = __builtin_amdgcn_mfma_f32_16x16x32_bf16(qh[s], cf, sa[ks2], 0, 0, 0);
        sa[ks2] = __builtin_amdgcn_mfma_f32_16x16x32_bf16(ql[s], cf, sa[ks2], 0, 0, 0);
      }
    }
    float sv[2][4], mxe[4];
#pragma unroll
    for (int e = 0; e < 4; ++e) {
#pragma unroll
      for (int ks2 = 0; ks2 < 2; ++ks2) {
        const int key = split * KPS + 32 * kg + 16 * ks2 + l15;
        float s = rbf(rbf(sa[ks2][e]) * SCALE);
        s = (key <= (SK - SQ)) ? s : -1e9f;
        sv[ks2][e] = s * LOG2E;
      }
      float t = fmaxf(sv[0][e], sv[1][e]);
      t = fmaxf(t, dppf<D_XOR1>(t)); t = fmaxf(t, dppf<D_XOR2>(t)); t = fmaxf(t, dppf<D_XOR7>(t)); t = fmaxf(t, dppf<D_XOR8>(t));
      mxe[e] = t;
      if (l15 == 0) MX[kg][16 * hh + 4 * g4 + e] = t;
    }
    __syncthreads();
#pragma unroll
    for (int e = 0; e < 4; ++e) {
      const int head = 16 * hh + 4 * g4 + e;
      const float M = fmaxf(fmaxf(MX[0][head], MX[1][head]), fmaxf(MX[2][head], MX[3][head]));
      float ps = 0.f;
#pragma unroll
      for (int ks2 = 0; ks2 < 2; ++ks2) {
        const float pe = exp2f(sv[ks2][e] - M);
        ps += pe;
        *reinterpret_cast<u16*>(P + head * PSTR + (32 * kg + 16 * ks2 + l15) * 2) = f2bf_bits(pe);
      }
      ps += dppf<D_XOR1>(ps); ps += dppf<D_XOR2>(ps); ps += dppf<D_XOR7>(ps); ps += dppf<D_XOR8>(ps);
      if (l15 == 0) LS[kg][head] = ps;
      (void)mxe;
    }
    __syncthreads();
    if (tid < NH) {
      const float M = fmaxf(fmaxf(MX[0][tid], MX[1][tid]), fmaxf(MX[2][tid], MX[3][tid]));
      const float L = ((LS[0][tid] + LS[1][tid]) + LS[2][tid]) + LS[3][tid];
      __hip_atomic_store(reinterpret_cast<unsigned long long*>(bml + (((size_t)b * SB + split) * NH + tid) * 2),
                         __builtin_bit_cast(unsigned long long, make_float2(M, L)), __ATOMIC_RELAXED, __HIP_MEMORY_SCOPE_AGENT);
    }
    f32x16 acc[2];
#pragma unroll
    for (int i = 0; i < 16; ++i) { acc[0][i] = 0.f; acc[1][i] = 0.f; }
    const u32 blk = (lane >> 4) & 1, q4 = (lane & 15) >> 2, p4 = lane & 3;
#pragma unroll
    for (int ksp = 0; ksp < 8; ++ksp) {
      const bf16x8 pf = *reinterpret_cast<const bf16x8*>(P + l31 * PSTR + 32 * ksp + 16 * hl);
      const char* img = tiles + ((ksp >> 1) * 4 + (wave >> 1)) * 8192;
      const int ks = ksp & 1;
#pragma unroll
      for (int cb2 = 0; cb2 < 2; ++cb2) {
        const u32 cc = (2 * wave + cb2) & 3;
        const u32 a0 = img_off(16 * ks + 8 * hl + q4, 4 * cc + 2 * blk + (p4 >> 1)) + 8 * (p4 & 1);
        const u32 a1 = img_off(16 * ks + 8 * hl + 4 + q4, 4 * cc + 2 * blk + (p4 >> 1)) + 8 * (p4 & 1);
        const s16x4 t0 = __builtin_amdgcn_ds_read_tr16_b64_v4i16((__attribute__((address_space(3))) s16x4*)(img + a0));
        const s16x4 t1 = __builtin_amdgcn_ds_read_tr16_b64_v4i16((__attribute__((address_space(3))) s16x4*)(img + a1));
        const __attribute__((ext_vector_type(8))) short tr = {t0[0], t0[1], t0[2], t0[3], t1[0], t1[1], t1[2], t1[3]};
        acc[cb2] = __builtin_amdgcn_mfma_f32_32x32x16_bf16(pf, __builtin_bit_cast(bf16x8, tr), acc[cb2], 0, 0, 0);
      }
    }
    __syncthreads();
    const __amdgpu_buffer_rsrc_t rs = __builtin_amdgcn_make_buffer_rsrc(bpo, 0, (int)BPO_BYTES, 0x00020000);
    float* stg = reinterpret_cast<float*>(tiles + wave * 8192);
#pragma unroll
    for (int i = 0; i < 16; ++i) {
      const int row = (i & 3) + 8 * (i >> 2) + 4 * hl;
      stg[row * 64 + l31] = acc[0][i]; stg[row * 64 + 32 + l31] = acc[1][i];
    }
    wave_sync();
#pragma unroll
    for (int k = 0; k < 8; ++k) {
      const int idx = lane + 64 * k, row = idx >> 4, c4 = idx & 15;
      st16_wt(rs, (u32)(((((b * SB + split) * NH + row) * DC) + 64 * wave + 4 * c4) * 4), *reinterpret_cast<const u32x4*>(&stg[row * 64 + 4 * c4]));
    }
    signal_count(cnt + CNT_BATT);
    STAMP(3);
  }
  {
    float (*xs)[512] = reinterpret_cast<float (*)[512]>(smem);
    float* wts = reinterpret_cast<float*>(smem + 4096);
    float* red = reinterpret_cast<float*>(smem + 8192);
    const int cq = tid & 7, rg = tid >> 3;
    u32x4 wv[8];
#pragma unroll
    for (int i = 0; i < 8; ++i) wv[i] = ld16nt(wuv + (size_t)(rg + 64 * i) * HD + h * DH + dh * 64 + 8 * cq);
    wait_count(cnt + CNT_BATT, NBW);
    STAMP(4);
    float pv[2][SB];
#pragma unroll
    for (int b = 0; b < 2; ++b)
#pragma unroll
      for (int s = 0; s < SB; ++s) pv[b][s] = bpo[(((size_t)b * SB + s) * NH + h) * DC + tid];
    if (tid < 64) {
      const int b = tid >> 5, s = tid & 31;
      const float2 ml = *reinterpret_cast<const float2*>(bml + (((size_t)b * SB + s) * NH + h) * 2);
      float M = ml.x;
#pragma unroll
      for (int o = 1; o < 32; o <<= 1) M = fmaxf(M, __shfl_xor(M, o, 32));
      const float w = exp2f(ml.x - M);
      float L = w * ml.y;
#pragma unroll
      for (int o = 1; o < 32; o <<= 1) L += __shfl_xor(L, o, 32);
      wts[tid] = w / L;
    }
    __syncthreads();
#pragma unroll
    for (int b = 0; b < 2; ++b) {
      float a = 0.f;
#pragma unroll
      for (int s = 0; s < SB; ++s) a = fmaf(wts[b * 32 + s], pv[b][s], a);
      xs[b][tid] = a;
    }
    __syncthreads();
    float acc[2][8];
#pragma unroll
    for (int k = 0; k < 8; ++k) { acc[0][k] = 0.f; acc[1][k] = 0.f; }
#pragma unroll
    for (int i = 0; i < 8; ++i) {
      const float x0 = xs[0][rg + 64 * i], x1 = xs[1][rg + 64 * i];
#pragma unroll
      for (int k = 0; k < 4; ++k) {
        const float a = lo16(wv[i][k]), bb = hi16(wv[i][k]);
        acc[0][2 * k] = fmaf(x0, a, acc[0][2 * k]); acc[0][2 * k + 1] = fmaf(x0, bb, acc[0][2 * k + 1]);
        acc[1][2 * k] = fmaf(x1, a, acc[1][2 * k]); acc[1][2 * k + 1] = fmaf(x1, bb, acc[1][2 * k + 1]);
      }
    }
#pragma unroll
    for (int b = 0; b < 2; ++b)
#pragma unroll
      for (int k = 0; k < 8; ++k) { float v = acc[b][k]; v += __shfl_xor(v, 8); v += __shfl_xor(v, 16); v += __shfl_xor(v, 32); acc[b][k] = v; }
    if (lane < 8) {
#pragma unroll
      for (int b = 0; b < 2; ++b)
#pragma unroll
        for (int k = 0; k < 8; ++k) red[(wave * 2 + b) * 64 + cq * 8 + k] = acc[b][k];
    }
    __syncthreads();
    if (tid < 128) {
      const int b = tid >> 6, d = tid & 63;
      float v = 0.f;
#pragma unroll
      for (int w = 0; w < NWV; ++w) v += red[(w * 2 + b) * 64 + d];
      ctx[b * HD + h * DH + dh * 64 + d] = rbf(v);
    }
    STAMP(5);
  }
}

__global__ __launch_bounds__(NTH, 2) void k_main(const u16* __restrict__ K, const u16* __restrict__ V, const u16* __restrict__ xA,
                                                 const u16* __restrict__ wqA, float* __restrict__ pml, float* __restrict__ po,
                                                 const u16* __restrict__ xB, const u16* __restrict__ wqB, const u16* __restrict__ wuk,
                                                 u16* __restrict__ qpl, const u16* __restrict__ C, float* __restrict__ bml, float* __restrict__ bpo,
                                                 const u16* __restrict__ wuv, float* __restrict__ ctx, u32* __restrict__ cnt DBG_PARAM) {
  __shared__ __attribute__((aligned(16))) char smem[LDS_MAIN_BYTES];
  if (blockIdx.x < NBW) role_B(blockIdx.x, xB, wqB, wuk, qpl, C, bml, bpo, wuv, ctx, cnt, smem DBG_ARG);
  else role_attnA(blockIdx.x - NBW, K, V, xA, wqA, pml, po, smem DBG_ARG);
}

__global__ __launch_bounds__(256) void k_out(const float* __restrict__ pml, const float* __restrict__ po, const float* __restrict__ ctx,
                                             const u16* __restrict__ woA, const u16* __restrict__ woB, float* __restrict__ outp,
                                             u32* __restrict__ cnt, __hip_bfloat16* __restrict__ out) {
  __shared__ float ys[2][DH];
  __shared__ __attribute__((aligned(16))) float red[4][2][128];
  __shared__ u32 last_flag;
  const int mat = blockIdx.x >> 7, h = (blockIdx.x >> 2) & 31, cb = blockIdx.x & 3;
  const int t = threadIdx.x, lane = t & 63, wave = t >> 6, cq = t & 15, rg = t >> 4;
  const u16* W = mat ? woB : woA;
  u32x4 wv[8];
#pragma unroll
  for (int i = 0; i < 8; ++i) wv[i] = ld16nt(W + (size_t)(h * DH + rg + 16 * i) * DM + cb * 128 + 8 * cq);
  {
    const int b = t >> 7, d = t & 127, bh = b * NH + h;
    float y;
    if (mat == 0) {
      float M = -INFINITY;
#pragma unroll
      for (int s = 0; s < SA; ++s) M = fmaxf(M, pml[((size_t)bh * SA + s) * 2]);
      float L = 0.f, o = 0.f;
#pragma unroll
      for (int s = 0; s < SA; ++s) {
        const float w = exp2f(pml[((size_t)bh * SA + s) * 2] - M);
        L = fmaf(w, pml[((size_t)bh * SA + s) * 2 + 1], L); o = fmaf(w, po[((size_t)bh * SA + s) * DH + d], o);
      }
      y = rbf(o / L);
    } else {
      y = ctx[b * HD + h * DH + d];
    }
    ys[b][d] = y;
  }
  __syncthreads();
  float acc[2][8];
#pragma unroll
  for (int k = 0; k < 8; ++k) { acc[0][k] = 0.f; acc[1][k] = 0.f; }
#pragma unroll
  for (int i = 0; i < 8; ++i) {
    const float y0 = ys[0][rg + 16 * i], y1 = ys[1][rg + 16 * i];
#pragma unroll
    for (int k = 0; k < 4; ++k) {
      const float a = lo16(wv[i][k]), bb = hi16(wv[i][k]);
      acc[0][2 * k] = fmaf(y0, a, acc[0][2 * k]); acc[0][2 * k + 1] = fmaf(y0, bb, acc[0][2 * k + 1]);
      acc[1][2 * k] = fmaf(y1, a, acc[1][2 * k]); acc[1][2 * k + 1] = fmaf(y1, bb, acc[1][2 * k + 1]);
    }
  }
#pragma unroll
  for (int b = 0; b < 2; ++b)
#pragma unroll
    for (int k = 0; k < 8; ++k) { float v = acc[b][k]; v += __shfl_xor(v, 16); v += __shfl_xor(v, 32); acc[b][k] = v; }
  if (lane < 16) {
#pragma unroll
    for (int b = 0; b < 2; ++b) {
      *reinterpret_cast<f32x4*>(&red[wave][b][8 * cq]) = f32x4{acc[b][0], acc[b][1], acc[b][2], acc[b][3]};
      *reinterpret_cast<f32x4*>(&red[wave][b][8 * cq + 4]) = f32x4{acc[b][4], acc[b][5], acc[b][6], acc[b][7]};
    }
  }
  __syncthreads();
  const __amdgpu_buffer_rsrc_t rs = __builtin_amdgcn_make_buffer_rsrc(outp, 0, (int)OUTP_BYTES, 0x00020000);
  if (t < 64) {
    const int b = t >> 5, c4 = t & 31;
    f32x4 v;
#pragma unroll
    for (int e = 0; e < 4; ++e) v[e] = ((red[0][b][4 * c4 + e] + red[1][b][4 * c4 + e]) + red[2][b][4 * c4 + e]) + red[3][b][4 * c4 + e];
    st16_wt(rs, (u32)((((((mat * 4 + cb) * NH + h) * NB + b) * 128) + 4 * c4) * 4), __builtin_bit_cast(u32x4, v));
  }
  asm volatile("s_waitcnt vmcnt(0)" ::: "memory");
  __syncthreads();
  if (t == 0) {
    const u32 old = __hip_atomic_fetch_add(cnt + CNT_TICKET + 64 * (mat * 4 + cb), 1u, __ATOMIC_RELAXED, __HIP_MEMORY_SCOPE_AGENT);
    const u32 last = (old == (u32)(NH - 1)) ? 1u : 0u;
    if (last) { __builtin_amdgcn_fence(__ATOMIC_ACQUIRE, "agent"); asm volatile("s_waitcnt vmcnt(0)" ::: "memory"); }
    last_flag = last;
  }
  __syncthreads();
  if (!last_flag) return;
  {
    const int b = t >> 7, n = t & 127;
    float s = 0.f;
#pragma unroll 8
    for (int hh = 0; hh < NH; ++hh) s += outp[((((size_t)(mat * 4 + cb) * NH + hh) * NB + b) * 128) + n];
    out[b * (2 * DM) + mat * DM + cb * 128 + n] = __float2bfloat16(s);
  }
}

extern "C" void kernel_launch(void* const* d_in, const int* in_sizes, int n_in, void* d_out, int out_size, void* d_ws, size_t ws_size,
                              hipStream_t stream) {
  (void)in_sizes; (void)n_in; (void)out_size;
  if (ws_size < WS_END) return;
  const u16* xA = (const u16*)d_in[0];  const u16* Kc = (const u16*)d_in[1]; const u16* Vc = (const u16*)d_in[2];
  const u16* wqA = (const u16*)d_in[3]; const u16* woA = (const u16*)d_in[4];
  const u16* xB = (const u16*)d_in[5];  const u16* Cc = (const u16*)d_in[6];
  const u16* wqB = (const u16*)d_in[7]; const u16* wuk = (const u16*)d_in[8]; const u16* wuv = (const u16*)d_in[9]; const u16* woB = (const u16*)d_in[10];
  char* ws = (char*)d_ws;
  u32* cnt = (u32*)(ws + WS_CNT);
  u16* qpl = (u16*)(ws + WS_QABS);
  float* aml = (float*)(ws + WS_AML); float* apo = (float*)(ws + WS_APO);
  float* bml = (float*)(ws + WS_BML); float* bpo = (float*)(ws + WS_BPO);
  float* ctx = (float*)(ws + WS_CTX); float* outp = (float*)(ws + WS_OUTP);
  k_zero<<<1, 256, 0, stream>>>(cnt);
  k_main<<<NBW + NB * NH * SA, NTH, 0, stream>>>(Kc, Vc, xA, wqA, aml, apo, xB, wqB, wuk, qpl, Cc, bml, bpo, wuv, ctx, cnt DBG_ARG);
  k_out<<<256, 256, 0, stream>>>(aml, apo, ctx, woA, woB, outp, cnt, (__hip_bfloat16*)d_out);
}
```
